```python
import torch

def _softmax(x, axis=-1):
    return torch.softmax(x, dim=axis)
def _gelu(x):
    return torch.nn.functional.gelu(x, approximate="tanh")
def _silu(x):
    return torch.nn.functional.silu(x)
def _sigmoid(x):
    return torch.sigmoid(x)
def _layernorm(x, axis=-1, eps=1e-5):
    m = x.mean(dim=axis, keepdim=True)
    v = x.var(dim=axis, keepdim=True, unbiased=False)
    return (x - m) / torch.sqrt(v + eps)
def _scatter_add(tgt, idx, src, axis=0):
    return tgt.clone().index_add_(axis, idx, src)
def _segment_sum(src, seg, n):
    out = torch.zeros((n,) + src.shape[1:], dtype=src.dtype)
    return out.index_add_(0, seg, src)
def _pad(x, pw):
    flat = tuple(v for pair in reversed(pw) for v in pair)
    return torch.nn.functional.pad(x, flat)

kernel_name = 'standalone__geglu_ffn__prompt'

def setup_inputs():

    _w = (lambda a: (lambda t: t.to(torch.bfloat16) if t.is_floating_point() else t)(torch.as_tensor(a)))
    B, S, D, F = 1, 2048, 640, 1728
    return {
        "x": _w((lambda *s: torch.randn(*s, dtype=torch.bfloat16) * 0.05)(B, S, D)),
        "w_gate": _w((lambda *s: torch.randn(*s, dtype=torch.bfloat16) * 0.05)(D, F)),
        "w_up": _w((lambda *s: torch.randn(*s, dtype=torch.bfloat16) * 0.05)(D, F)),
        "w_down": _w((lambda *s: torch.randn(*s, dtype=torch.bfloat16) * 0.05)(F, D)),
    }

def reference(x, w_gate, w_up, w_down):
    return (_gelu(x @ w_gate) * (x @ w_up)) @ w_down
```

```cpp
#include <hip/hip_runtime.h>
#include <hip/hip_bf16.h>

typedef short bf16x8 __attribute__((ext_vector_type(8)));
typedef short s16x4 __attribute__((ext_vector_type(4)));
typedef float f32x4 __attribute__((ext_vector_type(4)));
typedef unsigned u32x4 __attribute__((ext_vector_type(4)));
typedef unsigned u32x2 __attribute__((ext_vector_type(2)));
#define LAS __attribute__((address_space(3)))
typedef LAS s16x4* lds_s4p;
typedef LAS bf16x8* lds_s8p;
typedef unsigned short u16;

constexpr int S_ = 2048, D_ = 640, F_ = 1728;
constexpr int KS2 = F_ / 32;

#define WAIT_V(n) asm volatile("s_waitcnt vmcnt(%0)" ::"n"(n) : "memory")
#define WAIT_L(n) asm volatile("s_waitcnt lgkmcnt(%0)" ::"n"(n) : "memory")
#define BAR() __builtin_amdgcn_s_barrier()
#define SCHED() __builtin_amdgcn_sched_barrier(0)

__device__ __forceinline__ float bf16r(float f) {
    return __bfloat162float(__float2bfloat16(f));
}
__device__ __forceinline__ unsigned pack2(float lo, float hi) {
    __hip_bfloat16 a = __float2bfloat16(lo), b = __float2bfloat16(hi);
    return (unsigned)__builtin_bit_cast(u16, a) | ((unsigned)__builtin_bit_cast(u16, b) << 16);
}
__device__ __forceinline__ float geglu(float g, float u) {
    g = bf16r(g); u = bf16r(u);
    const float c0 = -2.0f * 0.7978845608028654f * 1.4426950408889634f;
    float t = g * g;
    float z = g * (c0 + (c0 * 0.044715f) * t);
    float e = __builtin_amdgcn_exp2f(z);
    float ge = g * __builtin_amdgcn_rcpf(1.0f + e);
    return bf16r(ge) * u;
}

constexpr int K1_STAGE = 49152;
constexpr int K1_NST = 3;
constexpr int K1_NT = D_ / 64;

__global__ __launch_bounds__(512, 2)
void geglu_k1(const u16* __restrict__ x, const u16* __restrict__ wg, const u16* __restrict__ wu,
              const u16* __restrict__ wd, u16* __restrict__ ht, u16* __restrict__ wdt) {
    extern __shared__ __attribute__((aligned(1024))) char smem[];
    const int tid = threadIdx.x, lane = tid & 63;
    const int wid = __builtin_amdgcn_readfirstlane(tid >> 6);
    const int xcd = blockIdx.x & 7, li = blockIdx.x >> 3;
    const int q = xcd & 3, mh = xcd >> 2;
    const int nf = (q == 3) ? 6 : 7;
    if (li >= nf * 4) {
        int slot = (q < 3) ? ((mh * 3 + q) * 4 + (li - 28)) : (24 + mh * 8 + (li - 24));
        int job = slot * 8 + wid;
        if (job >= KS2 * 5) return;
        int s = job / 5, c = job - s * 5;
        char* my = smem + wid * 8192;
        const u16* src = wd + (size_t)(s * 32 + (lane >> 4)) * D_ + c * 128 + (lane & 15) * 8;
#pragma unroll
        for (int i = 0; i < 8; ++i)
            __builtin_amdgcn_global_load_lds((const unsigned*)(src + i * 4 * D_), (LAS unsigned*)(my + i * 1024), 16, 0, 0);
        WAIT_V(0);
        const int g = lane >> 4, qq = (lane & 15) >> 2, p = lane & 3;
#pragma unroll
        for (int u = 0; u < 8; ++u) {
            s16x4 a = __builtin_amdgcn_ds_read_tr16_b64_v4i16((lds_s4p)(my + (4 * g + qq) * 256 + u * 32 + p * 8));
            s16x4 b = __builtin_amdgcn_ds_read_tr16_b64_v4i16((lds_s4p)(my + (16 + 4 * g + qq) * 256 + u * 32 + p * 8));
            bf16x8 v = __builtin_shufflevector(a, b, 0, 1, 2, 3, 4, 5, 6, 7);
            int un = c * 8 + u, sl = un / 5, ui = un - sl * 5;
            *(bf16x8*)(wdt + ((size_t)((sl * KS2 + s) * 5 + ui) * 64 + lane) * 8) = v;
        }
        return;
    }
    const int mi = li & 3, fi = li >> 2;
    const int m0 = (mh * 4 + mi) * 256;
    const int fs = q * 7 + fi;
    const int wm = wid >> 1, wn = wid & 1;

    const int r8 = lane >> 3, c8 = lane & 7;
    const u16* xsrc[4];
#pragma unroll
    for (int i = 0; i < 4; ++i) {
        int row = wid * 32 + i * 8 + r8;
        xsrc[i] = x + (size_t)(m0 + row) * D_ + ((c8 ^ ((row >> 1) & 7)) * 8);
    }
    const int krow = wid * 8 + r8;
    const int fsw = ((krow >> 1) & 1) | (((krow >> 3) & 1) << 1);
    const size_t woff = (size_t)krow * F_ + fs * 64 + ((c8 ^ (fsw << 1)) * 8);
    const u16* gsrc = wg + woff;
    const u16* usrc = wu + woff;

    auto stage = [&](int buf, int kt) {
        char* b = smem + buf * K1_STAGE;
#pragma unroll
        for (int i = 0; i < 4; ++i)
            __builtin_amdgcn_global_load_lds((const unsigned*)(xsrc[i] + kt * 64), (LAS unsigned*)(b + wid * 4096 + i * 1024), 16, 0, 0);
        __builtin_amdgcn_global_load_lds((const unsigned*)(gsrc + (size_t)kt * 64 * F_), (LAS unsigned*)(b + 32768 + wid * 1024), 16, 0, 0);
        __builtin_amdgcn_global_load_lds((const unsigned*)(usrc + (size_t)kt * 64 * F_), (LAS unsigned*)(b + 40960 + wid * 1024), 16, 0, 0);
    };

    const int fr = lane & 15, fq = lane >> 4;
    const int xoff0 = (wm * 64 + fr) * 128 + ((fq ^ (fr >> 1)) * 16);
    const int xoff1 = xoff0 ^ 64;
    const int tq = (lane & 15) >> 2, tp = lane & 3;
    const int tf = ((tq >> 1) & 1) | ((fq & 1) << 1);
    const int woff0 = 32768 + (8 * fq + tq) * 128 + (((2 * wn) ^ tf) * 32) + tp * 8;
    const int woff1 = woff0 ^ 32;

    f32x4 accg[4][2], accu[4][2];
#pragma unroll
    for (int m = 0; m < 4; ++m)
#pragma unroll
        for (int a = 0; a < 2; ++a) { accg[m][a] = (f32x4){0, 0, 0, 0}; accu[m][a] = (f32x4){0, 0, 0, 0}; }

    stage(0, 0);
    stage(1, 1);
    int cur = 0;
    for (int t = 0; t < K1_NT; ++t) {
        if (t + 1 < K1_NT) WAIT_V(6); else WAIT_V(0);
        WAIT_L(0);
        BAR();
        int nb = cur + 2; if (nb >= 3) nb -= 3;
        if (t + 2 < K1_NT) stage(nb, t + 2);
        const char* b = smem + cur * K1_STAGE;
#pragma unroll
        for (int ks = 0; ks < 2; ++ks) {
            bf16x8 xf[4], gf[2], uf[2];
#pragma unroll
            for (int a = 0; a < 2; ++a) {
                const char* wp = b + (a ? woff1 : woff0) + ks * 4096;
                s16x4 g0 = __builtin_amdgcn_ds_read_tr16_b64_v4i16((lds_s4p)(wp));
                s16x4 g1 = __builtin_amdgcn_ds_read_tr16_b64_v4i16((lds_s4p)(wp + 512));
                s16x4 u0 = __builtin_amdgcn_ds_read_tr16_b64_v4i16((lds_s4p)(wp + 8192));
                s16x4 u1 = __builtin_amdgcn_ds_read_tr16_b64_v4i16((lds_s4p)(wp + 8192 + 512));
                gf[a] = __builtin_shufflevector(g0, g1, 0, 1, 2, 3, 4, 5, 6, 7);
                uf[a] = __builtin_shufflevector(u0, u1, 0, 1, 2, 3, 4, 5, 6, 7);
            }
#pragma unroll
            for (int m = 0; m < 4; ++m)
                xf[m] = *(const bf16x8*)(b + (ks ? xoff1 : xoff0) + m * 2048);
#pragma unroll
            for (int m = 0; m < 4; ++m)
#pragma unroll
                for (int a = 0; a < 2; ++a) {
                    accg[m][a] = __builtin_amdgcn_mfma_f32_16x16x32_bf16(gf[a], xf[m], accg[m][a], 0, 0, 0);
                    accu[m][a] = __builtin_amdgcn_mfma_f32_16x16x32_bf16(uf[a], xf[m], accu[m][a], 0, 0, 0);
                }
        }
        cur = cur + 1; if (cur >= 3) cur = 0;
    }
    {
        const int msl = (m0 >> 6) + wm, kst = fs * 2 + wn;
        u16* dst = ht + ((size_t)(msl * KS2 + kst) * 4 * 64 + lane) * 8;
#pragma unroll
        for (int m = 0; m < 4; ++m) {
            u32x4 v;
            v[0] = pack2(geglu(accg[m][0][0], accu[m][0][0]), geglu(accg[m][0][1], accu[m][0][1]));
            v[1] = pack2(geglu(accg[m][0][2], accu[m][0][2]), geglu(accg[m][0][3], accu[m][0][3]));
            v[2] = pack2(geglu(accg[m][1][0], accu[m][1][0]), geglu(accg[m][1][1], accu[m][1][1]));
            v[3] = pack2(geglu(accg[m][1][2], accu[m][1][2]), geglu(accg[m][1][3], accu[m][1][3]));
            *(u32x4*)(dst + m * 512) = v;
        }
    }
}

constexpr int K2_D = 3;

__global__ __launch_bounds__(256, 1)
void geglu_k2(const u16* __restrict__ ht, const u16* __restrict__ wdt, u16* __restrict__ out) {
    __shared__ __attribute__((aligned(16))) f32x4 red[4][15][64];
    const int tid = threadIdx.x, lane = tid & 63;
    const int wid = __builtin_amdgcn_readfirstlane(tid >> 6);
    const int xcd = blockIdx.x & 7, li = blockIdx.x >> 3;
    const int msl = (xcd >> 1) * 8 + (li & 7);
    const int nsl = (xcd & 1) * 4 + (li >> 3);
    const int s0 = wid * 13 + (wid < 2 ? wid : 2);
    const bf16x8* hp = (const bf16x8*)(ht + ((size_t)(msl * KS2 + s0) * 4 * 64 + lane) * 8);
    const bf16x8* wp = (const bf16x8*)(wdt + ((size_t)(nsl * KS2 + s0) * 5 * 64 + lane) * 8);

    f32x4 acc[4][5];
#pragma unroll
    for (int m = 0; m < 4; ++m)
#pragma unroll
        for (int n = 0; n < 5; ++n) acc[m][n] = (f32x4){0, 0, 0, 0};

    bf16x8 hf[K2_D][4], wf[K2_D][5];
#define K2_LOAD(slot, s) do { _Pragma("unroll") for (int n = 0; n < 5; ++n) wf[slot][n] = wp[(s) * 320 + n * 64]; \
                              _Pragma("unroll") for (int m = 0; m < 4; ++m) hf[slot][m] = hp[(s) * 256 + m * 64]; } while (0)
#define K2_MMA(slot) do { _Pragma("unroll") for (int m = 0; m < 4; ++m) _Pragma("unroll") for (int n = 0; n < 5; ++n) \
        acc[m][n] = __builtin_amdgcn_mfma_f32_16x16x32_bf16(wf[slot][n], hf[slot][m], acc[m][n], 0, 0, 0); } while (0)
#pragma unroll
    for (int d = 0; d < K2_D; ++d) K2_LOAD(d, d);
#pragma unroll
    for (int s = 0; s < 13; ++s) {
        K2_MMA(s % K2_D);
        if (s + K2_D < 13) K2_LOAD(s % K2_D, s + K2_D);
        else if (s + K2_D == 13) { if (wid < 2) K2_LOAD(s % K2_D, 13); }
    }
    if (wid < 2) K2_MMA(13 % K2_D);

#pragma unroll
    for (int m = 0; m < 4; ++m) {
        if (m == wid) continue;
        const int slot = (m < wid) ? m : m - 1;
#pragma unroll
        for (int n = 0; n < 5; ++n) red[wid][slot * 5 + n][lane] = acc[m][n];
    }
    __syncthreads();
    f32x4 o[5];
#pragma unroll
    for (int n = 0; n < 5; ++n) {
        o[n] = wid == 0 ? acc[0][n] : wid == 1 ? acc[1][n] : wid == 2 ? acc[2][n] : acc[3][n];
    }
#pragma unroll
    for (int w = 0; w < 4; ++w) {
        if (w == wid) continue;
        const int slot = (wid < w) ? wid : wid - 1;
#pragma unroll
        for (int n = 0; n < 5; ++n) o[n] += red[w][slot * 5 + n][lane];
    }
    const int fr = lane & 15, fq = lane >> 4;
    u16* dst = out + (size_t)(msl * 64 + wid * 16 + fr) * D_ + nsl * 80 + fq * 4;
#pragma unroll
    for (int n = 0; n < 5; ++n) {
        u32x2 v; v[0] = pack2(o[n][0], o[n][1]); v[1] = pack2(o[n][2], o[n][3]);
        *(u32x2*)(dst + n * 16) = v;
    }
}

extern "C" void kernel_launch(void* const* d_in, const int* in_sizes, int n_in,
                              void* d_out, int out_size, void* d_ws, size_t ws_size,
                              hipStream_t stream) {
    const u16* x = (const u16*)d_in[0];
    const u16* wg = (const u16*)d_in[1];
    const u16* wu = (const u16*)d_in[2];
    const u16* wd = (const u16*)d_in[3];
    u16* ht = (u16*)d_ws;
    u16* wdt = (u16*)((char*)d_ws + (size_t)8 * 1024 * 1024);
    (void)hipFuncSetAttribute((const void*)geglu_k1, hipFuncAttributeMaxDynamicSharedMemorySize, K1_NST * K1_STAGE);
    geglu_k1<<<256, 512, K1_NST * K1_STAGE, stream>>>(x, wg, wu, wd, ht, wdt);
    geglu_k2<<<256, 256, 0, stream>>>(ht, wdt, (u16*)d_out);
}
```

```cpp
#include <hip/hip_runtime.h>
#include <hip/hip_bf16.h>

typedef short bf16x8 __attribute__((ext_vector_type(8)));
typedef short s16x4 __attribute__((ext_vector_type(4)));
typedef float f32x4 __attribute__((ext_vector_type(4)));
typedef float f32x2 __attribute__((ext_vector_type(2)));
typedef unsigned u32x4 __attribute__((ext_vector_type(4)));
typedef unsigned u32x2 __attribute__((ext_vector_type(2)));
#define LAS __attribute__((address_space(3)))
typedef LAS s16x4* lds_s4p;
typedef unsigned short u16;

constexpr int S_ = 2048, D_ = 640, F_ = 1728;
constexpr int KS2 = F_ / 32;

#define WAIT_V(n) asm volatile("s_waitcnt vmcnt(%0)" ::"n"(n) : "memory")
#define WAIT_L(n) asm volatile("s_waitcnt lgkmcnt(%0)" ::"n"(n) : "memory")
#define BAR() do { asm volatile("" ::: "memory"); __builtin_amdgcn_s_barrier(); asm volatile("" ::: "memory"); } while (0)
#define SCHED() __builtin_amdgcn_sched_barrier(0)

__device__ __forceinline__ void glds16(unsigned voff, const void* sbase, unsigned lds_addr) {
    asm volatile("s_mov_b32 m0, %2\n\ts_nop 0\n\tglobal_load_lds_dwordx4 %0, %1" ::"v"(voff), "s"(sbase), "s"(lds_addr) : "memory");
}
#define ST_FLAGS "sc0 sc1"
__device__ __forceinline__ void store16_wt(void* p, u32x4 v) {
    asm volatile("global_store_dwordx4 %0, %1, off " ST_FLAGS "\n\ts_nop 1" ::"v"(p), "v"(v) : "memory");
}
__device__ __forceinline__ unsigned pack2(float lo, float hi) {
    __hip_bfloat16 a = __float2bfloat16(lo), b = __float2bfloat16(hi);
    return (unsigned)__builtin_bit_cast(u16, a) | ((unsigned)__builtin_bit_cast(u16, b) << 16);
}
__device__ __forceinline__ unsigned geglu2(float g0, float g1, float u0, float u1) {
    const float c0 = -2.0f * 0.7978845608028654f * 1.4426950408889634f;
    f32x2 g = {g0, g1}, u = {u0, u1};
    f32x2 t = g * g;
    f32x2 z = g * (t * (c0 * 0.044715f) + c0);
    f32x2 d; d.x = __builtin_amdgcn_exp2f(z.x); d.y = __builtin_amdgcn_exp2f(z.y);
    d = d + 1.0f;
    f32x2 r; r.x = __builtin_amdgcn_rcpf(d.x); r.y = __builtin_amdgcn_rcpf(d.y);
    f32x2 h = (g * u) * r;
    return pack2(h.x, h.y);
}
__device__ __forceinline__ int fresh_lane() {
    int l = __builtin_amdgcn_mbcnt_hi(~0u, __builtin_amdgcn_mbcnt_lo(~0u, 0u));
    asm volatile("" : "+v"(l));
    return l;
}

constexpr int K1_STAGE = 49152;
constexpr int K1_NT = D_ / 64;
constexpr int LDS_TOUCH = 3 * K1_STAGE;
constexpr int LDS_BYTES = LDS_TOUCH + 3072;
constexpr int PF0 = 3;


__global__ __launch_bounds__(768, 3)
void geglu_k1(const u16* __restrict__ x, const u16* __restrict__ wg, const u16* __restrict__ wu,
              const u16* __restrict__ wd, u16* __restrict__ ht, u16* __restrict__ wdt) {
    extern __shared__ __attribute__((aligned(1024))) char smem[];
    const int tid = threadIdx.x, lane = tid & 63;
    const int wid = __builtin_amdgcn_readfirstlane(tid >> 6);
    const int bid = blockIdx.x;
    const int xcd = bid & 7, li = bid >> 3;
    const int q = xcd & 3, mh = xcd >> 2;
    const int nf = (q == 3) ? 6 : 7;
    const unsigned lbase = (unsigned)(size_t)(LAS char*)smem;
    const bool is_gemm = li < nf * 4;

#define TOUCH_U(nexpr, kk) do { int n_ = (nexpr); if (n_ > 5503) n_ = 5503; const char* tb_; \
        if (n_ < 2048) tb_ = (const char*)x + (size_t)n_ * 1280 + (kk) * 128; \
        else { int i2 = n_ - 2048; int mat = i2 >= 1728; i2 -= mat * 1728; \
               tb_ = (const char*)(mat ? wu : wg) + (size_t)(kk) * 64 * 3456 + (size_t)i2 * 128; } \
        asm volatile("s_mov_b32 m0, %1\n\ts_nop 0\n\tglobal_load_lds_dword %0, off" :: "v"(tb_), "s"(lbase + LDS_TOUCH + wid * 256) : "memory"); } while (0)

    if (!is_gemm) {
        const int slot = (q < 3) ? ((mh * 3 + q) * 4 + (li - 28)) : (24 + mh * 8 + (li - 24));
        for (int kk = PF0; kk < 10; ++kk) {
            const int w3 = wid - ((kk - PF0) & 3) * 3;
            if (w3 >= 0 && w3 < 3) { const int l_ = fresh_lane(); TOUCH_U(slot * 138 + w3 * 46 + (l_ < 46 ? l_ : 45), kk); }
        }
        WAIT_V(0);
        BAR();
        const int rn = slot & 7, rk = slot >> 3;
        const int nks = rk < 4 ? 11 : 10;
        if (wid < nks) {
            char* my = smem + wid * 5120;
            const int g = lane >> 4, qq = (lane & 15) >> 2, p = lane & 3;
            const int s = rk * 11 + wid;
            const u16* src = wd + (size_t)(s * 32) * D_ + rn * 80;
#pragma unroll
            for (int i = 0; i < 5; ++i) {
                int c = i * 64 + lane, row = c / 10, ch = c - row * 10;
                glds16(row * (D_ * 2) + ch * 16, src, lbase + wid * 5120 + i * 1024);
            }
            WAIT_V(0);
#pragma unroll
            for (int u = 0; u < 5; ++u) {
                s16x4 a = __builtin_amdgcn_ds_read_tr16_b64_v4i16((lds_s4p)(my + (4 * g + qq) * 160 + u * 32 + p * 8));
                s16x4 b = __builtin_amdgcn_ds_read_tr16_b64_v4i16((lds_s4p)(my + (16 + 4 * g + qq) * 160 + u * 32 + p * 8));
                bf16x8 v = __builtin_shufflevector(a, b, 0, 1, 2, 3, 4, 5, 6, 7);
                store16_wt(wdt + ((size_t)((rn * KS2 + s) * 5 + u) * 64 + lane) * 8, __builtin_bit_cast(u32x4, v));
            }
        }
        return;
    }
    const int mi = li & 3, fi = li >> 2;
    const int mslice1 = mh * 4 + mi;
    const int m0 = mslice1 * 256;
    const int fs = q * 7 + fi;

    if (wid >= 8) {
        const int lg = wid & 3;
        const int r8 = lane >> 3, c8 = lane & 7;
        const unsigned xvoffA = (lg * 64 + r8) * (D_ * 2) + ((c8 ^ (r8 >> 1)) * 16);
        const unsigned xvoffB = (lg * 64 + 8 + r8) * (D_ * 2) + ((c8 ^ (4 + (r8 >> 1))) * 16);
        const unsigned wvoff0 = ((lg & 1) * 32 + r8) * (F_ * 2) + ((c8 ^ ((((r8 >> 1) & 1)) << 1)) * 16);
        const unsigned wvoff1 = ((lg & 1) * 32 + 8 + r8) * (F_ * 2) + ((c8 ^ ((((r8 >> 1) & 1) | 2) << 1)) * 16);
        const u16* xb = x + (size_t)m0 * D_;
        const u16* wb = (lg >= 2 ? wu : wg) + fs * 64;
#define K1_STAGE_ISSUE(buf, kt) do { \
        const unsigned l_ = lbase + (buf) * K1_STAGE; \
        _Pragma("unroll") for (int j_ = 0; j_ < 2; ++j_) { \
            glds16(wvoff0, wb + (size_t)((kt) * 64 + j_ * 16) * F_, l_ + 32768 + lg * 4096 + j_ * 2048); \
            glds16(wvoff1, wb + (size_t)((kt) * 64 + j_ * 16) * F_, l_ + 32768 + lg * 4096 + j_ * 2048 + 1024); } \
        _Pragma("unroll") for (int i_ = 0; i_ < 4; ++i_) { \
            glds16(xvoffA, xb + (kt) * 64 + i_ * 16 * D_, l_ + lg * 8192 + i_ * 2048); \
            glds16(xvoffB, xb + (kt) * 64 + i_ * 16 * D_, l_ + lg * 8192 + i_ * 2048 + 1024); } } while (0)
        K1_STAGE_ISSUE(0, 0);
        BAR();
        WAIT_V(0);
        BAR();
        K1_STAGE_ISSUE(1, 1);
        K1_STAGE_ISSUE(2, 2);
        int bf = 0;
#pragma unroll 1
        for (int t = 0; t < 7; ++t) {
            WAIT_V(12);
            BAR();
            K1_STAGE_ISSUE(bf, t + 3);
            bf = bf == 2 ? 0 : bf + 1;
        }
        WAIT_V(12); BAR();
        WAIT_V(0); BAR();
        return;
    }
    const int fr = lane & 15, fq = lane >> 4;
    const int wm = wid >> 1, wn = wid & 1;
    const int xoff0 = (wm * 64 + fr) * 128 + ((fq ^ (fr >> 1)) * 16);
    const int xoff1 = xoff0 ^ 64;
    const int tq = (lane & 15) >> 2, tp = lane & 3;
    const int tf = ((tq >> 1) & 1) | ((fq & 1) << 1);
    const int woff0 = 32768 + (8 * fq + tq) * 128 + (((2 * wn) ^ tf) * 32) + tp * 8;
    const int woff1 = woff0 ^ 32;
    f32x4 accg[4][2], accu[4][2];
#pragma unroll
    for (int m = 0; m < 4; ++m)
#pragma unroll
        for (int a = 0; a < 2; ++a) { accg[m][a] = (f32x4){0, 0, 0, 0}; accu[m][a] = (f32x4){0, 0, 0, 0}; }
    bf16x8 xf[2][4], gf[2][2], uf[2][2];
#define K1_READ(set, buf, ks) do { \
    const char* b_ = smem + (buf) * K1_STAGE; \
    _Pragma("unroll") for (int a = 0; a < 2; ++a) { \
        const char* wp = b_ + (a ? woff1 : woff0) + (ks) * 4096; \
        s16x4 g0 = __builtin_amdgcn_ds_read_tr16_b64_v4i16((lds_s4p)(wp)); \
        s16x4 g1 = __builtin_amdgcn_ds_read_tr16_b64_v4i16((lds_s4p)(wp + 512)); \
        s16x4 u0 = __builtin_amdgcn_ds_read_tr16_b64_v4i16((lds_s4p)(wp + 8192)); \
        s16x4 u1 = __builtin_amdgcn_ds_read_tr16_b64_v4i16((lds_s4p)(wp + 8192 + 512)); \
        gf[set][a] = __builtin_shufflevector(g0, g1, 0, 1, 2, 3, 4, 5, 6, 7); \
        uf[set][a] = __builtin_shufflevector(u0, u1, 0, 1, 2, 3, 4, 5, 6, 7); } \
    _Pragma("unroll") for (int m = 0; m < 4; ++m) \
        xf[set][m] = *(const bf16x8*)(b_ + ((ks) ? xoff1 : xoff0) + m * 2048); } while (0)
#define K1_MMA(set) do { \
    _Pragma("unroll") for (int m = 0; m < 4; ++m) \
    _Pragma("unroll") for (int a = 0; a < 2; ++a) { \
        accg[m][a] = __builtin_amdgcn_mfma_f32_16x16x32_bf16(gf[set][a], xf[set][m], accg[m][a], 0, 0, 0); \
        accu[m][a] = __builtin_amdgcn_mfma_f32_16x16x32_bf16(uf[set][a], xf[set][m], accu[m][a], 0, 0, 0); } } while (0)
#define ILV() do { _Pragma("unroll") for (int i_ = 0; i_ < 12; ++i_) { \
    __builtin_amdgcn_sched_group_barrier(0x8, 1, 0); __builtin_amdgcn_sched_group_barrier(0x100, 1, 0); } \
    __builtin_amdgcn_sched_group_barrier(0x8, 4, 0); } while (0)
    BAR();
    if (wid < PF0 - 1) { const int l_ = fresh_lane(); const int gi = mslice1 * 27 + fs; TOUCH_U(gi * 26 + (l_ < 26 ? l_ : 25), wid + 1); }
    BAR();
    K1_READ(0, 0, 0);
#pragma unroll
    for (int t = 0; t < K1_NT; ++t) {
        const int cur = t % 3, nxt = (t + 1) % 3;
        SCHED();
        K1_READ(1, cur, 1);
        K1_MMA(0);
        ILV();
        SCHED();
        if (t + 1 < K1_NT) {
            WAIT_L(0);
            BAR();
            SCHED();
            K1_READ(0, nxt, 0);
            K1_MMA(1);
            ILV();
        } else {
            K1_MMA(1);
        }
        SCHED();
    }
    const int hsl = (m0 >> 6) + wm, kst = fs * 2 + wn;
    u16* dst = ht + ((size_t)(hsl * KS2 + kst) * 4 * 64 + lane) * 8;
#pragma unroll
    for (int m = 0; m < 4; ++m) {
        u32x4 v;
        v[0] = geglu2(accg[m][0][0], accg[m][0][1], accu[m][0][0], accu[m][0][1]);
        v[1] = geglu2(accg[m][0][2], accg[m][0][3], accu[m][0][2], accu[m][0][3]);
        v[2] = geglu2(accg[m][1][0], accg[m][1][1], accu[m][1][0], accu[m][1][1]);
        v[3] = geglu2(accg[m][1][2], accg[m][1][3], accu[m][1][2], accu[m][1][3]);
        store16_wt(dst + m * 512, v);
    }
}

constexpr int K2_D = 3;

__global__ __launch_bounds__(256, 1)
void geglu_k2(const u16* __restrict__ ht, const u16* __restrict__ wdt, u16* __restrict__ out) {
    __shared__ __attribute__((aligned(16))) f32x4 red[4][3][5][64];
    const int tid = threadIdx.x, lane = tid & 63;
    const int wid = __builtin_amdgcn_readfirstlane(tid >> 6);
    const int xcd = blockIdx.x & 7, li = blockIdx.x >> 3;
    const int msl = (xcd >> 1) * 8 + (li & 7);
    const int nsl = (xcd & 1) * 4 + (li >> 3);
    const int s0 = wid * 13 + (wid < 2 ? wid : 2);
    const u16* hb = ht + (size_t)(msl * KS2 + s0) * 4 * 64 * 8;
    const u16* wb = wdt + (size_t)(nsl * KS2 + s0) * 5 * 64 * 8;
    const unsigned voff = lane * 16 + 2048;
    unsigned hvoff[4];
#pragma unroll
    for (int ml = 0; ml < 4; ++ml) hvoff[ml] = lane * 16 + ((wid + ml) & 3) * 1024;

    f32x4 acc[4][5];
#pragma unroll
    for (int m = 0; m < 4; ++m)
#pragma unroll
        for (int n = 0; n < 5; ++n) acc[m][n] = (f32x4){0, 0, 0, 0};

    bf16x8 hf[K2_D][4], wf[K2_D][5];
#define GLD(dst, vo, sb, imm) asm volatile("global_load_dwordx4 %0, %1, %2 offset:%3" : "=v"(dst) : "v"(vo), "s"(sb), "i"(imm))
#define KMAP(s) (s)
#define K2_LOAD(slot, s) do { const int sm_ = KMAP(s); const u16* w_ = wb + sm_ * 2560; const u16* h_ = hb + sm_ * 2048; \
        GLD(wf[slot][0], voff, w_, -2048); GLD(hf[slot][0], hvoff[0], h_, 0); GLD(wf[slot][1], voff, w_, -1024); GLD(hf[slot][1], hvoff[1], h_, 0); \
        GLD(wf[slot][2], voff, w_, 0); GLD(hf[slot][2], hvoff[2], h_, 0); GLD(wf[slot][3], voff, w_, 1024); GLD(hf[slot][3], hvoff[3], h_, 0); \
        GLD(wf[slot][4], voff, w_, 2048); } while (0)
#define K2_WAIT(slot, n) asm volatile("s_waitcnt vmcnt(%9)" : "+v"(wf[slot][0]), "+v"(wf[slot][1]), "+v"(wf[slot][2]), "+v"(wf[slot][3]), "+v"(wf[slot][4]), \
        "+v"(hf[slot][0]), "+v"(hf[slot][1]), "+v"(hf[slot][2]), "+v"(hf[slot][3]) : "n"(n))
#define K2_MMA(slot) do { _Pragma("unroll") for (int m = 0; m < 4; ++m) _Pragma("unroll") for (int n = 0; n < 5; ++n) \
        acc[m][n] = __builtin_amdgcn_mfma_f32_16x16x32_bf16(wf[slot][n], hf[slot][m], acc[m][n], 0, 0, 0); } while (0)
#define K2_RUN(NS) do { \
    bf16x8 hf[K2_D][4], wf[K2_D][5]; \
    _Pragma("unroll") for (int d = 0; d < K2_D; ++d) K2_LOAD(d, d); \
    _Pragma("unroll") for (int s = 0; s < (NS); ++s) { \
        const int rem = (NS) - 1 - s; \
        K2_WAIT(s % K2_D, 9 * (rem < K2_D - 1 ? rem : K2_D - 1)); \
        K2_MMA(s % K2_D); \
        SCHED(); \
        if (s + K2_D < (NS)) K2_LOAD(s % K2_D, s + K2_D); \
    } } while (0)
    if (wid < 2) K2_RUN(14); else K2_RUN(13);
#pragma unroll
    for (int ml = 1; ml < 4; ++ml)
#pragma unroll
        for (int n = 0; n < 5; ++n) red[(wid + ml) & 3][ml - 1][n][lane] = acc[ml][n];
    __syncthreads();
    const int fr = lane & 15, fq = lane >> 4;
    u16* dst = out + (size_t)(msl * 64 + wid * 16 + fr) * D_ + nsl * 80 + fq * 4;
#pragma unroll
    for (int n = 0; n < 5; ++n) {
        f32x4 o = acc[0][n] + red[wid][0][n][lane] + red[wid][1][n][lane] + red[wid][2][n][lane];
        u32x2 v; v[0] = pack2(o[0], o[1]); v[1] = pack2(o[2], o[3]);
        *(u32x2*)(dst + n * 16) = v;
    }
}

extern "C" void kernel_launch(void* const* d_in, const int* in_sizes, int n_in,
                              void* d_out, int out_size, void* d_ws, size_t ws_size,
                              hipStream_t stream) {
    const u16* x = (const u16*)d_in[0];
    const u16* wg = (const u16*)d_in[1];
    const u16* wu = (const u16*)d_in[2];
    const u16* wd = (const u16*)d_in[3];
    u16* ht = (u16*)d_ws;
    u16* wdt = (u16*)((char*)d_ws + (size_t)8 * 1024 * 1024);
    (void)hipFuncSetAttribute((const void*)geglu_k1, hipFuncAttributeMaxDynamicSharedMemorySize, LDS_BYTES);
    geglu_k1<<<256, 768, LDS_BYTES, stream>>>(x, wg, wu, wd, ht, wdt);
    geglu_k2<<<256, 256, 0, stream>>>(ht, wdt, (u16*)d_out);
}
```

```cpp
#include <hip/hip_runtime.h>
#include <hip/hip_bf16.h>

typedef short bf16x8 __attribute__((ext_vector_type(8)));
typedef short s16x4 __attribute__((ext_vector_type(4)));
typedef float f32x4 __attribute__((ext_vector_type(4)));
typedef float f32x2 __attribute__((ext_vector_type(2)));
typedef unsigned u32x4 __attribute__((ext_vector_type(4)));
typedef unsigned u32x2 __attribute__((ext_vector_type(2)));
#define LAS __attribute__((address_space(3)))
typedef LAS s16x4* lds_s4p;
typedef unsigned short u16;

constexpr int S_ = 2048, D_ = 640, F_ = 1728;
constexpr int KS2 = F_ / 32;

#define WAIT_V(n) asm volatile("s_waitcnt vmcnt(%0)" ::"n"(n) : "memory")
#define WAIT_L(n) asm volatile("s_waitcnt lgkmcnt(%0)" ::"n"(n) : "memory")
#define BAR() do { asm volatile("" ::: "memory"); __builtin_amdgcn_s_barrier(); asm volatile("" ::: "memory"); } while (0)
#define SCHED() __builtin_amdgcn_sched_barrier(0)

__device__ __forceinline__ void glds16(unsigned voff, const void* sbase, unsigned lds_addr) {
    asm volatile("s_mov_b32 m0, %2\n\ts_nop 0\n\tglobal_load_lds_dwordx4 %0, %1" ::"v"(voff), "s"(sbase), "s"(lds_addr) : "memory");
}
#define ST_FLAGS "sc0 sc1"
__device__ __forceinline__ void store16_wt(void* p, u32x4 v) {
    asm volatile("global_store_dwordx4 %0, %1, off " ST_FLAGS "\n\ts_nop 1" ::"v"(p), "v"(v) : "memory");
}
__device__ __forceinline__ unsigned pack2(float lo, float hi) {
    __hip_bfloat16 a = __float2bfloat16(lo), b = __float2bfloat16(hi);
    return (unsigned)__builtin_bit_cast(u16, a) | ((unsigned)__builtin_bit_cast(u16, b) << 16);
}
__device__ __forceinline__ unsigned geglu2(float g0, float g1, float u0, float u1) {
    const float c0 = -2.0f * 0.7978845608028654f * 1.4426950408889634f;
    f32x2 g = {g0, g1}, u = {u0, u1};
    f32x2 t = g * g;
    f32x2 z = g * (t * (c0 * 0.044715f) + c0);
    f32x2 d; d.x = __builtin_amdgcn_exp2f(z.x); d.y = __builtin_amdgcn_exp2f(z.y);
    d = d + 1.0f;
    f32x2 r; r.x = __builtin_amdgcn_rcpf(d.x); r.y = __builtin_amdgcn_rcpf(d.y);
    f32x2 h = (g * r) * u;
    return pack2(h.x, h.y);
}
__device__ __forceinline__ int fresh_lane() {
    int l = __builtin_amdgcn_mbcnt_hi(~0u, __builtin_amdgcn_mbcnt_lo(~0u, 0u));
    asm volatile("" : "+v"(l));
    return l;
}

constexpr int K1_STAGE = 49152;
constexpr int K1_NT = D_ / 64;
constexpr int LDS_TOUCH = 3 * K1_STAGE;
constexpr int LDS_BYTES = LDS_TOUCH + 3072;
constexpr int PF0 = 3;


__global__ __launch_bounds__(768, 3)
void geglu_k1(const u16* __restrict__ x, const u16* __restrict__ wg, const u16* __restrict__ wu,
              const u16* __restrict__ wd, u16* __restrict__ ht, u16* __restrict__ wdt) {
    extern __shared__ __attribute__((aligned(1024))) char smem[];
    const int tid = threadIdx.x, lane = tid & 63;
    const int wid = __builtin_amdgcn_readfirstlane(tid >> 6);
    const int bid = blockIdx.x;
    const int xcd = bid & 7, li = bid >> 3;
    const int q = xcd & 3, mh = xcd >> 2;
    const int nf = (q == 3) ? 6 : 7;
    const unsigned lbase = (unsigned)(size_t)(LAS char*)smem;
    const bool is_gemm = li < nf * 4;

#define TOUCH_U(nexpr, kk) do { int n_ = (nexpr); if (n_ > 5503) n_ = 5503; const char* tb_; \
        if (n_ < 2048) tb_ = (const char*)x + (size_t)n_ * 1280 + (kk) * 128; \
        else { int i2 = n_ - 2048; int mat = i2 >= 1728; i2 -= mat * 1728; \
               tb_ = (const char*)(mat ? wu : wg) + (size_t)(kk) * 64 * 3456 + (size_t)i2 * 128; } \
        asm volatile("s_mov_b32 m0, %1\n\ts_nop 0\n\tglobal_load_lds_dword %0, off" :: "v"(tb_), "s"(lbase + LDS_TOUCH + wid * 256) : "memory"); } while (0)

    if (!is_gemm) {
        const int slot = (q < 3) ? ((mh * 3 + q) * 4 + (li - 28)) : (24 + mh * 8 + (li - 24));
        for (int kk = PF0; kk < 10; ++kk) {
            const int w3 = wid - ((kk - PF0) & 3) * 3;
            if (w3 >= 0 && w3 < 3) { const int l_ = fresh_lane(); TOUCH_U(slot * 138 + w3 * 46 + (l_ < 46 ? l_ : 45), kk); }
        }
        WAIT_V(0);
        BAR();
        const int rn = slot & 7, rk = slot >> 3;
        const int nks = rk < 4 ? 11 : 10;
        if (wid < nks) {
            char* my = smem + wid * 5120;
            const int g = lane >> 4, qq = (lane & 15) >> 2, p = lane & 3;
            const int s = rk * 11 + wid;
            const u16* src = wd + (size_t)(s * 32) * D_ + rn * 80;
#pragma unroll
            for (int i = 0; i < 5; ++i) {
                int c = i * 64 + lane, row = c / 10, ch = c - row * 10;
                glds16(row * (D_ * 2) + ch * 16, src, lbase + wid * 5120 + i * 1024);
            }
            WAIT_V(0);
#pragma unroll
            for (int u = 0; u < 5; ++u) {
                s16x4 a = __builtin_amdgcn_ds_read_tr16_b64_v4i16((lds_s4p)(my + (4 * g + qq) * 160 + u * 32 + p * 8));
                s16x4 b = __builtin_amdgcn_ds_read_tr16_b64_v4i16((lds_s4p)(my + (16 + 4 * g + qq) * 160 + u * 32 + p * 8));
                bf16x8 v = __builtin_shufflevector(a, b, 0, 1, 2, 3, 4, 5, 6, 7);
                store16_wt(wdt + ((size_t)((rn * KS2 + s) * 5 + u) * 64 + lane) * 8, __builtin_bit_cast(u32x4, v));
            }
        }
        return;
    }
    const int mi = li & 3, fi = li >> 2;
    const int mslice1 = mh * 4 + mi;
    const int m0 = mslice1 * 256;
    const int fs = q * 7 + fi;

    if (wid >= 8) {
        const int lg = wid & 3;
        const int r8 = lane >> 3, c8 = lane & 7;
        const unsigned xvoffA = (lg * 64 + r8) * (D_ * 2) + ((c8 ^ (r8 >> 1)) * 16);
        const unsigned xvoffB = (lg * 64 + 8 + r8) * (D_ * 2) + ((c8 ^ (4 + (r8 >> 1))) * 16);
        const unsigned wvoff0 = ((lg & 1) * 32 + r8) * (F_ * 2) + ((c8 ^ ((((r8 >> 1) & 1)) << 1)) * 16);
        const unsigned wvoff1 = ((lg & 1) * 32 + 8 + r8) * (F_ * 2) + ((c8 ^ ((((r8 >> 1) & 1) | 2) << 1)) * 16);
        const u16* xb = x + (size_t)m0 * D_;
        const u16* wb = (lg >= 2 ? wu : wg) + fs * 64;
#define K1_STAGE_ISSUE(buf, kt) do { \
        const unsigned l_ = lbase + (buf) * K1_STAGE; \
        _Pragma("unroll") for (int j_ = 0; j_ < 2; ++j_) { \
            glds16(wvoff0, wb + (size_t)((kt) * 64 + j_ * 16) * F_, l_ + 32768 + lg * 4096 + j_ * 2048); \
            glds16(wvoff1, wb + (size_t)((kt) * 64 + j_ * 16) * F_, l_ + 32768 + lg * 4096 + j_ * 2048 + 1024); } \
        _Pragma("unroll") for (int i_ = 0; i_ < 4; ++i_) { \
            glds16(xvoffA, xb + (kt) * 64 + i_ * 16 * D_, l_ + lg * 8192 + i_ * 2048); \
            glds16(xvoffB, xb + (kt) * 64 + i_ * 16 * D_, l_ + lg * 8192 + i_ * 2048 + 1024); } } while (0)
        K1_STAGE_ISSUE(0, 0);
        BAR();
        WAIT_V(0);
        BAR();
        K1_STAGE_ISSUE(1, 1);
        K1_STAGE_ISSUE(2, 2);
        int bf = 0;
#pragma unroll 1
        for (int t = 0; t < 7; ++t) {
            WAIT_V(12);
            BAR();
            K1_STAGE_ISSUE(bf, t + 3);
            bf = bf == 2 ? 0 : bf + 1;
        }
        WAIT_V(12); BAR();
        WAIT_V(0); BAR();
        return;
    }
    const int fr = lane & 15, fq = lane >> 4;
    const int wm = wid >> 1, wn = wid & 1;
    const int xoff0 = (wm * 64 + fr) * 128 + ((fq ^ (fr >> 1)) * 16);
    const int xoff1 = xoff0 ^ 64;
    const int tq = (lane & 15) >> 2, tp = lane & 3;
    const int tf = ((tq >> 1) & 1) | ((fq & 1) << 1);
    const int woff0 = 32768 + (8 * fq + tq) * 128 + (((2 * wn) ^ tf) * 32) + tp * 8;
    const int woff1 = woff0 ^ 32;
    f32x4 accg[4][2], accu[4][2];
#pragma unroll
    for (int m = 0; m < 4; ++m)
#pragma unroll
        for (int a = 0; a < 2; ++a) { accg[m][a] = (f32x4){0, 0, 0, 0}; accu[m][a] = (f32x4){0, 0, 0, 0}; }
    bf16x8 xf[2][4], gf[2][2], uf[2][2];
#define K1_READ(set, buf, ks) do { \
    const char* b_ = smem + (buf) * K1_STAGE; \
    _Pragma("unroll") for (int a = 0; a < 2; ++a) { \
        const char* wp = b_ + (a ? woff1 : woff0) + (ks) * 4096; \
        s16x4 g0 = __builtin_amdgcn_ds_read_tr16_b64_v4i16((lds_s4p)(wp)); \
        s16x4 g1 = __builtin_amdgcn_ds_read_tr16_b64_v4i16((lds_s4p)(wp + 512)); \
        s16x4 u0 = __builtin_amdgcn_ds_read_tr16_b64_v4i16((lds_s4p)(wp + 8192)); \
        s16x4 u1 = __builtin_amdgcn_ds_read_tr16_b64_v4i16((lds_s4p)(wp + 8192 + 512)); \
        gf[set][a] = __builtin_shufflevector(g0, g1, 0, 1, 2, 3, 4, 5, 6, 7); \
        uf[set][a] = __builtin_shufflevector(u0, u1, 0, 1, 2, 3, 4, 5, 6, 7); } \
    _Pragma("unroll") for (int m = 0; m < 4; ++m) \
        xf[set][m] = *(const bf16x8*)(b_ + ((ks) ? xoff1 : xoff0) + m * 2048); } while (0)
#define K1_MMA(set) do { \
    _Pragma("unroll") for (int m = 0; m < 4; ++m) \
    _Pragma("unroll") for (int a = 0; a < 2; ++a) { \
        accg[m][a] = __builtin_amdgcn_mfma_f32_16x16x32_bf16(gf[set][a], xf[set][m], accg[m][a], 0, 0, 0); \
        accu[m][a] = __builtin_amdgcn_mfma_f32_16x16x32_bf16(uf[set][a], xf[set][m], accu[m][a], 0, 0, 0); } } while (0)
#define ILV() do { _Pragma("unroll") for (int i_ = 0; i_ < 12; ++i_) { \
    __builtin_amdgcn_sched_group_barrier(0x8, 1, 0); __builtin_amdgcn_sched_group_barrier(0x100, 1, 0); } \
    __builtin_amdgcn_sched_group_barrier(0x8, 4, 0); } while (0)
    BAR();
    if (wid < PF0 - 1) { const int l_ = fresh_lane(); const int gi = mslice1 * 27 + fs; TOUCH_U(gi * 26 + (l_ < 26 ? l_ : 25), wid + 1); }
    BAR();
    K1_READ(0, 0, 0);
#pragma unroll
    for (int t = 0; t < K1_NT; ++t) {
        const int cur = t % 3, nxt = (t + 1) % 3;
        SCHED();
        K1_READ(1, cur, 1);
        K1_MMA(0);
        ILV();
        SCHED();
        if (t + 1 < K1_NT) {
            if (t == 5) WAIT_V(0);
            WAIT_L(0);
            BAR();
            SCHED();
            K1_READ(0, nxt, 0);
            K1_MMA(1);
            ILV();
        } else {
            K1_MMA(1);
        }
        SCHED();
    }
    const int hsl = (m0 >> 6) + wm, kst = fs * 2 + wn;
    u16* dst = ht + ((size_t)(hsl * KS2 + kst) * 4 * 64 + lane) * 8;
#pragma unroll
    for (int m = 0; m < 4; ++m) {
        u32x4 v;
        v[0] = geglu2(accg[m][0][0], accg[m][0][1], accu[m][0][0], accu[m][0][1]);
        v[1] = geglu2(accg[m][0][2], accg[m][0][3], accu[m][0][2], accu[m][0][3]);
        v[2] = geglu2(accg[m][1][0], accg[m][1][1], accu[m][1][0], accu[m][1][1]);
        v[3] = geglu2(accg[m][1][2], accg[m][1][3], accu[m][1][2], accu[m][1][3]);
        store16_wt(dst + m * 512, v);
    }
}

constexpr int K2_D = 3;

__global__ __launch_bounds__(256, 1)
void geglu_k2(const u16* __restrict__ ht, const u16* __restrict__ wdt, u16* __restrict__ out) {
    __shared__ __attribute__((aligned(16))) f32x4 red[4][3][5][64];
    const int tid = threadIdx.x, lane = tid & 63;
    const int wid = __builtin_amdgcn_readfirstlane(tid >> 6);
    const int xcd = blockIdx.x & 7, li = blockIdx.x >> 3;
    const int msl = (xcd >> 1) * 8 + (li & 7);
    const int nsl = (xcd & 1) * 4 + (li >> 3);
    const int s0 = wid * 13 + (wid < 2 ? wid : 2);
    const u16* hb = ht + (size_t)(msl * KS2 + s0) * 4 * 64 * 8;
    const u16* wb = wdt + (size_t)(nsl * KS2 + s0) * 5 * 64 * 8;
    const unsigned voff = lane * 16 + 2048;
    unsigned hvoff[4];
#pragma unroll
    for (int ml = 0; ml < 4; ++ml) hvoff[ml] = lane * 16 + ((wid + ml) & 3) * 1024;

    f32x4 acc[4][5];
#pragma unroll
    for (int m = 0; m < 4; ++m)
#pragma unroll
        for (int n = 0; n < 5; ++n) acc[m][n] = (f32x4){0, 0, 0, 0};

#define GLD(dst, vo, sb, imm) asm volatile("global_load_dwordx4 %0, %1, %2 offset:%3" : "=v"(dst) : "v"(vo), "s"(sb), "i"(imm))
#define K2_LOAD(slot, s) do { const u16* w_ = wb + (s) * 2560; const u16* h_ = hb + (s) * 2048; \
        GLD(wf[slot][0], voff, w_, -2048); GLD(hf[slot][0], hvoff[0], h_, 0); GLD(wf[slot][1], voff, w_, -1024); GLD(hf[slot][1], hvoff[1], h_, 0); \
        GLD(wf[slot][2], voff, w_, 0); GLD(hf[slot][2], hvoff[2], h_, 0); GLD(wf[slot][3], voff, w_, 1024); GLD(hf[slot][3], hvoff[3], h_, 0); \
        GLD(wf[slot][4], voff, w_, 2048); } while (0)
#define K2_WAIT(slot, n) asm volatile("s_waitcnt vmcnt(%9)" : "+v"(wf[slot][0]), "+v"(wf[slot][1]), "+v"(wf[slot][2]), "+v"(wf[slot][3]), "+v"(wf[slot][4]), \
        "+v"(hf[slot][0]), "+v"(hf[slot][1]), "+v"(hf[slot][2]), "+v"(hf[slot][3]) : "n"(n))
#define K2_MMA(slot) do { _Pragma("unroll") for (int m = 0; m < 4; ++m) _Pragma("unroll") for (int n = 0; n < 5; ++n) \
        acc[m][n] = __builtin_amdgcn_mfma_f32_16x16x32_bf16(wf[slot][n], hf[slot][m], acc[m][n], 0, 0, 0); } while (0)
#define K2_RUN(NS) do { \
    bf16x8 hf[K2_D][4], wf[K2_D][5]; \
    _Pragma("unroll") for (int d = 0; d < K2_D; ++d) K2_LOAD(d, d); \
    _Pragma("unroll") for (int s = 0; s < (NS); ++s) { \
        const int rem = (NS) - 1 - s; \
        K2_WAIT(s % K2_D, 9 * (rem < K2_D - 1 ? rem : K2_D - 1)); \
        K2_MMA(s % K2_D); \
        SCHED(); \
        if (s + K2_D < (NS)) K2_LOAD(s % K2_D, s + K2_D); \
    } } while (0)
    if (wid < 2) K2_RUN(14); else K2_RUN(13);
#pragma unroll
    for (int ml = 1; ml < 4; ++ml)
#pragma unroll
        for (int n = 0; n < 5; ++n) red[(wid + ml) & 3][ml - 1][n][lane] = acc[ml][n];
    __syncthreads();
    const int fr = lane & 15, fq = lane >> 4;
    u16* dst = out + (size_t)(msl * 64 + wid * 16 + fr) * D_ + nsl * 80 + fq * 4;
#pragma unroll
    for (int n = 0; n < 5; ++n) {
        f32x4 o = acc[0][n] + red[wid][0][n][lane] + red[wid][1][n][lane] + red[wid][2][n][lane];
        u32x2 v; v[0] = pack2(o[0], o[1]); v[1] = pack2(o[2], o[3]);
        *(u32x2*)(dst + n * 16) = v;
    }
}

extern "C" void kernel_launch(void* const* d_in, const int* in_sizes, int n_in,
                              void* d_out, int out_size, void* d_ws, size_t ws_size,
                              hipStream_t stream) {
    const u16* x = (const u16*)d_in[0];
    const u16* wg = (const u16*)d_in[1];
    const u16* wu = (const u16*)d_in[2];
    const u16* wd = (const u16*)d_in[3];
    u16* ht = (u16*)d_ws;
    u16* wdt = (u16*)((char*)d_ws + (size_t)8 * 1024 * 1024);
    (void)hipFuncSetAttribute((const void*)geglu_k1, hipFuncAttributeMaxDynamicSharedMemorySize, LDS_BYTES);
    geglu_k1<<<256, 768, LDS_BYTES, stream>>>(x, wg, wu, wd, ht, wdt);
    geglu_k2<<<256, 256, 0, stream>>>(ht, wdt, (u16*)d_out);
}
```
